```python
import torch

def _softmax(x, axis=-1):
    return torch.softmax(x, dim=axis)
def _gelu(x):
    return torch.nn.functional.gelu(x, approximate="tanh")
def _silu(x):
    return torch.nn.functional.silu(x)
def _sigmoid(x):
    return torch.sigmoid(x)
def _layernorm(x, axis=-1, eps=1e-5):
    m = x.mean(dim=axis, keepdim=True)
    v = x.var(dim=axis, keepdim=True, unbiased=False)
    return (x - m) / torch.sqrt(v + eps)
def _scatter_add(tgt, idx, src, axis=0):
    return tgt.clone().index_add_(axis, idx, src)
def _segment_sum(src, seg, n):
    out = torch.zeros((n,) + src.shape[1:], dtype=src.dtype)
    return out.index_add_(0, seg, src)
def _pad(x, pw):
    flat = tuple(v for pair in reversed(pw) for v in pair)
    return torch.nn.functional.pad(x, flat)

kernel_name = 'standalone__linear_kernel_rfa__decode'

def setup_inputs():

    _w = (lambda a: (lambda t: t.to(torch.bfloat16) if t.is_floating_point() else t)(torch.as_tensor(a)))
    B, H, Sq, Sk, D, M = 2, 32, 1, 4096, 128, 8
    return {
        "q": _w((lambda *s: torch.randn(*s, dtype=torch.bfloat16) * 0.4)(B, H, Sq, D)),
        "k": _w((lambda *s: torch.randn(*s, dtype=torch.bfloat16) * 1.6)(B, H, Sk, D)),
        "v": _w((lambda *s: torch.randn(*s, dtype=torch.bfloat16) * 1.6)(B, H, Sk, D)),
        "omega": _w((lambda *s: torch.randn(*s, dtype=torch.bfloat16) * 1.6)(M, D)),
    }

def reference(q, k, v, omega):
    d = q.shape[-1]
    m = omega.shape[0]
    scale = float(d) ** -0.25
    qs = q * scale
    ks = k * scale
    q_proj = torch.einsum("bhsd,md->bhsm", qs, omega)
    k_proj = torch.einsum("bhsd,md->bhsm", ks, omega)
    q_sq = 0.5 * torch.einsum("bhsd,bhsd->bhs", qs, qs)[..., None]
    k_sq = 0.5 * torch.einsum("bhsd,bhsd->bhs", ks, ks)[..., None]
    inv_sqrt_m = float(m) ** -0.5
    phi_q = torch.exp(q_proj - q_sq) * inv_sqrt_m + 1e-6
    phi_k = torch.exp(k_proj - k_sq) * inv_sqrt_m + 1e-6
    kv = torch.einsum("bhsm,bhsd->bhmd", phi_k, v)
    z  = torch.einsum("bhsm->bhm", phi_k)
    num = torch.einsum("bhqm,bhmd->bhqd", phi_q, kv)
    den = torch.einsum("bhqm,bhm->bhq", phi_q, z)[..., None] + 1e-6
    return num / den
```

```cpp
#include <hip/hip_runtime.h>
#include <hip/hip_bf16.h>
#include <cstdint>

#define MIMIC 0
#define NSPLIT 8
#define NWAVES 4
#define USE_NT 0

constexpr int B_ = 2, H_ = 32, SK = 4096, D_ = 128, M_ = 8, BH = B_ * H_;
constexpr int KPS = SK / NSPLIT;
constexpr int KPW = KPS / NWAVES;
constexpr int NIT = KPW / 32;
static_assert(KPW % 32 == 0, "");

using u32x4 = __attribute__((ext_vector_type(4))) unsigned;
using f32x4 = __attribute__((ext_vector_type(4))) float;
using bf16x8 = __attribute__((ext_vector_type(8))) __bf16;
using bf16x2 = __attribute__((ext_vector_type(2))) __bf16;

__device__ __forceinline__ u32x4 ldg16(const void* p) {
  return *reinterpret_cast<const u32x4*>(p);
}
__device__ __forceinline__ float dot2(unsigned a, unsigned b, float acc) {
  return __builtin_amdgcn_fdot2_f32_bf16(__builtin_bit_cast(bf16x2, a), __builtin_bit_cast(bf16x2, b), acc, false);
}
__device__ __forceinline__ f32x4 mfma16(u32x4 a, u32x4 b, f32x4 c) {
  return __builtin_amdgcn_mfma_f32_16x16x32_bf16(__builtin_bit_cast(bf16x8, a), __builtin_bit_cast(bf16x8, b), c, 0, 0, 0);
}
__device__ __forceinline__ float bflo(unsigned u) { return __builtin_bit_cast(float, u << 16); }
__device__ __forceinline__ float bfhi(unsigned u) { return __builtin_bit_cast(float, u & 0xffff0000u); }
__device__ __forceinline__ float rbf(float x) {
  __bf16 h = (__bf16)x; return (float)h;
}
__device__ __forceinline__ unsigned scale_pair(unsigned u, float s) {
  bf16x2 r; r[0] = (__bf16)(bflo(u) * s); r[1] = (__bf16)(bfhi(u) * s);
  return __builtin_bit_cast(unsigned, r);
}
__device__ __forceinline__ u32x4 scale8(u32x4 v, float s) {
  u32x4 r; r[0] = scale_pair(v[0], s); r[1] = scale_pair(v[1], s); r[2] = scale_pair(v[2], s); r[3] = scale_pair(v[3], s); return r;
}

constexpr float SCALE = 0.29730177875068026f;
constexpr float INV_SQRT_M = 0.35355339059327373f;
constexpr float LOG2E = 1.4426950408889634f;

__device__ __forceinline__ f32x4 phi4(f32x4 proj, float nrm) {
  f32x4 r;
  const float sq = (0.5f * SCALE * SCALE * LOG2E) * nrm;
#pragma unroll
  for (int j = 0; j < 4; ++j) {
    float a = proj[j] * (SCALE * LOG2E) - sq;
    r[j] = __builtin_amdgcn_exp2f(a) * INV_SQRT_M + 1e-6f;
  }
  return r;
}

__device__ __forceinline__ float sum_x16(float x) {
  auto s = __builtin_amdgcn_permlane16_swap(__float_as_uint(x), __float_as_uint(x), false, false);
  return __uint_as_float(s[0]) + __uint_as_float(s[1]);
}
__device__ __forceinline__ float sum_x32(float x) {
  auto s = __builtin_amdgcn_permlane32_swap(__float_as_uint(x), __float_as_uint(x), false, false);
  return __uint_as_float(s[0]) + __uint_as_float(s[1]);
}

__global__ void __launch_bounds__(NWAVES * 64)
rfa_partial(const unsigned short* __restrict__ q, const unsigned short* __restrict__ k,
            const unsigned short* __restrict__ v, const unsigned short* __restrict__ om,
            float* __restrict__ part) {
  __shared__ __attribute__((aligned(16))) float lds[NWAVES * 32 + NWAVES * 132];
  const int tid = threadIdx.x, lane = tid & 63, wave = __builtin_amdgcn_readfirstlane(tid >> 6);
  const int bh = blockIdx.x / NSPLIT, sp = blockIdx.x % NSPLIT;
  const int n = lane & 15, g = lane >> 4;

  u32x4 A1[4], A2[4], Q[4];
#pragma unroll
  for (int kk = 0; kk < 4; ++kk) {
    u32x4 o = *reinterpret_cast<const u32x4*>(om + (n & 7) * D_ + (4 * kk + g) * 8);
    Q[kk] = *reinterpret_cast<const u32x4*>(q + bh * D_ + (4 * kk + g) * 8);
    const u32x4 z = {0u, 0u, 0u, 0u};
    A1[kk] = (n < 8) ? o : z;
    A2[kk] = (n < 8) ? z : o;
  }
  const size_t base = ((size_t)bh * SK + (size_t)sp * KPS + (size_t)wave * KPW) * D_;
  const unsigned short* kp = k + base + n * D_ + g * 8;
  const unsigned short* vp = v + base + (g * 8) * D_ + n * 8;

  f32x4 phq;
  {
    f32x4 d = {0.f, 0.f, 0.f, 0.f}; float nr = 0.f;
#pragma unroll
    for (int kk = 0; kk < 4; ++kk) {
      d = mfma16(A1[kk], Q[kk], d); d = mfma16(A2[kk], Q[kk], d);
#pragma unroll
      for (int j = 0; j < 4; ++j) nr = dot2(Q[kk][j], Q[kk][j], nr);
    }
    nr = sum_x32(sum_x16(nr));
    phq = phi4(d, nr);
  }

  float acc[8] = {0.f, 0.f, 0.f, 0.f, 0.f, 0.f, 0.f, 0.f};
  float den = 0.f;
  float* wl = lds + wave * 32;
  for (int it = 0; it < NIT; ++it) {
    u32x4 K1[4], K2[4], V[8];
#pragma unroll
    for (int kk = 0; kk < 4; ++kk) { K1[kk] = ldg16(kp + kk * 32); K2[kk] = ldg16(kp + 16 * D_ + kk * 32); }
#pragma unroll
    for (int i = 0; i < 8; ++i) V[i] = ldg16(vp + i * D_);
    kp += 32 * D_; vp += 32 * D_;
    f32x4 d = {0.f, 0.f, 0.f, 0.f}; float n1 = 0.f, n2 = 0.f;
#pragma unroll
    for (int kk = 0; kk < 4; ++kk) {
      d = mfma16(A1[kk], K1[kk], d); d = mfma16(A2[kk], K2[kk], d);
#pragma unroll
      for (int j = 0; j < 4; ++j) { n1 = dot2(K1[kk][j], K1[kk][j], n1); n2 = dot2(K2[kk][j], K2[kk][j], n2); }
    }
    auto s = __builtin_amdgcn_permlane32_swap(__float_as_uint(n1), __float_as_uint(n2), false, false);
    float nr = sum_x16(__uint_as_float(s[0]) + __uint_as_float(s[1]));
    f32x4 ph = phi4(d, nr);
    float w = ph[0] * phq[0] + ph[1] * phq[1] + ph[2] * phq[2] + ph[3] * phq[3];
    w = sum_x16(w);
    den += w;
    if ((g & 1) == 0) wl[n + 16 * (g >> 1)] = w;
    asm volatile("s_waitcnt lgkmcnt(0)" ::: "memory");
    const f32x4 w0 = *reinterpret_cast<const f32x4*>(wl + g * 8);
    const f32x4 w1 = *reinterpret_cast<const f32x4*>(wl + g * 8 + 4);
#pragma unroll
    for (int i = 0; i < 8; ++i) {
      const float wi = i < 4 ? w0[i & 3] : w1[i & 3];
#pragma unroll
      for (int j = 0; j < 4; ++j) {
        acc[2 * j] = fmaf(wi, bflo(V[i][j]), acc[2 * j]);
        acc[2 * j + 1] = fmaf(wi, bfhi(V[i][j]), acc[2 * j + 1]);
      }
    }
    asm volatile("s_waitcnt lgkmcnt(0)" ::: "memory");
  }
#pragma unroll
  for (int j = 0; j < 8; ++j) acc[j] = sum_x32(sum_x16(acc[j]));
  den = sum_x32(den);
#pragma unroll
  for (int o = 1; o < 16; o <<= 1) den += __shfl_xor(den, o, 64);
  float* red = lds + NWAVES * 32 + wave * 132;
  if (g == 0) {
    *reinterpret_cast<f32x4*>(red + n * 8) = f32x4{acc[0], acc[1], acc[2], acc[3]};
    *reinterpret_cast<f32x4*>(red + n * 8 + 4) = f32x4{acc[4], acc[5], acc[6], acc[7]};
    if (n == 0) red[128] = den;
  }
  __syncthreads();
  if (tid < 129) {
    float s = 0.f;
#pragma unroll
    for (int w = 0; w < NWAVES; ++w) s += lds[NWAVES * 32 + w * 132 + tid];
    part[(size_t)blockIdx.x * 132 + tid] = s;
  }
}

__global__ void __launch_bounds__(128)
rfa_combine(const float* __restrict__ part, __hip_bfloat16* __restrict__ out) {
  const int bh = blockIdx.x, d = threadIdx.x;
  float num = 0.f, den = 0.f;
#pragma unroll
  for (int s = 0; s < NSPLIT; ++s) {
    num += part[(size_t)(bh * NSPLIT + s) * 132 + d];
    den += part[(size_t)(bh * NSPLIT + s) * 132 + 128];
  }
  out[bh * D_ + d] = __float2bfloat16(num / (den + 1e-6f));
}

extern "C" void kernel_launch(void* const* d_in, const int* in_sizes, int n_in,
                              void* d_out, int out_size, void* d_ws, size_t ws_size,
                              hipStream_t stream) {
  const unsigned short* q = (const unsigned short*)d_in[0];
  const unsigned short* k = (const unsigned short*)d_in[1];
  const unsigned short* v = (const unsigned short*)d_in[2];
  const unsigned short* om = (const unsigned short*)d_in[3];
  float* part = (float*)d_ws;
  rfa_partial<<<BH * NSPLIT, NWAVES * 64, 0, stream>>>(q, k, v, om, part);
  rfa_combine<<<BH, 128, 0, stream>>>(part, (__hip_bfloat16*)d_out);
}
```

```cpp
#include <hip/hip_runtime.h>
#include <hip/hip_bf16.h>
#include <cstdint>

constexpr int B_ = 2, H_ = 32, SK = 4096, D_ = 128, BH = B_ * H_;
constexpr int NSPLIT = 4;
constexpr int NWAVES = 4;
constexpr int RING = 1;
constexpr int KPS = SK / NSPLIT;
constexpr int NSTEP = KPS / NWAVES / 16;
constexpr int SSTRIDE = 4096 * NWAVES;
constexpr int SLOT_G = 144;
constexpr size_t WS_SLOTS = 8192;
constexpr int AUX_NT = 2;

using u32x4 = __attribute__((ext_vector_type(4))) unsigned;
using f32x4 = __attribute__((ext_vector_type(4))) float;
using bf16x8 = __attribute__((ext_vector_type(8))) __bf16;
using bf16x2 = __attribute__((ext_vector_type(2))) __bf16;
typedef __attribute__((address_space(1))) unsigned gu32;
typedef __attribute__((address_space(1))) unsigned long long gu64;
#define RLX_AGENT __ATOMIC_RELAXED, __HIP_MEMORY_SCOPE_AGENT

__device__ __forceinline__ float dot2(unsigned a, unsigned b, float acc) {
  return __builtin_amdgcn_fdot2_f32_bf16(__builtin_bit_cast(bf16x2, a), __builtin_bit_cast(bf16x2, b), acc, false);
}
__device__ __forceinline__ f32x4 mfma16(u32x4 a, u32x4 b, f32x4 c) {
  return __builtin_amdgcn_mfma_f32_16x16x32_bf16(__builtin_bit_cast(bf16x8, a), __builtin_bit_cast(bf16x8, b), c, 0, 0, 0);
}
__device__ __forceinline__ float bflo(unsigned u) { return __builtin_bit_cast(float, u << 16); }
__device__ __forceinline__ float bfhi(unsigned u) { return __builtin_bit_cast(float, u & 0xffff0000u); }
__device__ __forceinline__ float rbf(float x) { __bf16 h = (__bf16)x; return (float)h; }
__device__ __forceinline__ unsigned scale_pair(unsigned u, float s) {
  bf16x2 r; r[0] = (__bf16)(bflo(u) * s); r[1] = (__bf16)(bfhi(u) * s);
  return __builtin_bit_cast(unsigned, r);
}
__device__ __forceinline__ u32x4 scale8(u32x4 v, float s) {
  u32x4 r; r[0] = scale_pair(v[0], s); r[1] = scale_pair(v[1], s); r[2] = scale_pair(v[2], s); r[3] = scale_pair(v[3], s);
  return r;
}

constexpr float SCALE = 0.29730177875068026f;
constexpr float INV_SQRT_M = 0.35355339059327373f;
constexpr float LOG2E = 1.4426950408889634f;

__device__ __forceinline__ float phi1(float proj, float sq) {
  float a = rbf(rbf(proj) - sq);
  float e = rbf(__builtin_amdgcn_exp2f(a * LOG2E));
  e = rbf(e * INV_SQRT_M);
  return rbf(e + 1e-6f);
}
__device__ __forceinline__ float half_sq(float nrm) { return 0.5f * rbf(nrm); }
__device__ __forceinline__ float sum_x16(float x) {
  auto s = __builtin_amdgcn_permlane16_swap(__float_as_uint(x), __float_as_uint(x), false, false);
  return __uint_as_float(s[0]) + __uint_as_float(s[1]);
}
__device__ __forceinline__ float sum_x32(float x) {
  auto s = __builtin_amdgcn_permlane32_swap(__float_as_uint(x), __float_as_uint(x), false, false);
  return __uint_as_float(s[0]) + __uint_as_float(s[1]);
}
__device__ __forceinline__ float sum_g(float x) { return sum_x32(sum_x16(x)); }

__global__ void __launch_bounds__(NWAVES * 64, 1)
rfa_fused(const unsigned short* __restrict__ q, const unsigned short* __restrict__ k,
          const unsigned short* __restrict__ v, const unsigned short* __restrict__ om,
          unsigned char* __restrict__ ws, unsigned short* __restrict__ out) {
  __shared__ __attribute__((aligned(16))) float lds[NWAVES * 16 * RING + NWAVES * 132];
  const int tid = threadIdx.x, lane = tid & 63, wave = __builtin_amdgcn_readfirstlane(tid >> 6);
  const int bh = blockIdx.x / NSPLIT, sp = blockIdx.x % NSPLIT;
  const int n = lane & 15, g = lane >> 4;

  const size_t base = ((size_t)bh * SK + (size_t)sp * KPS + (size_t)wave * 16) * D_;
  const auto krs = __builtin_amdgcn_make_buffer_rsrc((void*)(k + base), 0, (NSTEP - 1) * SSTRIDE + 4096, 0x00020000);
  const auto vrs = __builtin_amdgcn_make_buffer_rsrc((void*)(v + base), 0, (NSTEP - 1) * SSTRIDE + 4096, 0x00020000);
  const int koff = n * 256 + g * 16;
  const int voff = lane * 16;

  u32x4 A[4], Q[4];
  const int feat = 2 * (n >> 2) + (n & 1);
#pragma unroll
  for (int kk = 0; kk < 4; ++kk) {
    A[kk] = *reinterpret_cast<const u32x4*>(om + feat * D_ + (4 * kk + g) * 8);
    Q[kk] = *reinterpret_cast<const u32x4*>(q + bh * D_ + (4 * kk + g) * 8);
  }
  gu32* ep = (gu32*)(ws + (size_t)bh * 128);
  const unsigned tag = __hip_atomic_load(ep, RLX_AGENT) + 1u;
  __builtin_amdgcn_sched_barrier(0);
  u32x4 K[RING][4], V[RING][4];
#pragma unroll
  for (int r = 0; r < RING; ++r) {
#pragma unroll
    for (int kk = 0; kk < 4; ++kk) K[r][kk] = __builtin_amdgcn_raw_buffer_load_b128(krs, koff + kk * 64, r * SSTRIDE, AUX_NT);
#pragma unroll
    for (int i = 0; i < 4; ++i) V[r][i] = __builtin_amdgcn_raw_buffer_load_b128(vrs, voff + i * 1024, r * SSTRIDE, AUX_NT);
  }
  __builtin_amdgcn_sched_barrier(0);

  float pq0, pq1;
  {
    f32x4 d = {0.f, 0.f, 0.f, 0.f}; float nr = 0.f;
#pragma unroll
    for (int kk = 0; kk < 4; ++kk) {
      const u32x4 s = scale8(Q[kk], SCALE);
      d = mfma16(A[kk], s, d);
#pragma unroll
      for (int j = 0; j < 4; ++j) nr = dot2(s[j], s[j], nr);
    }
    const float sq = half_sq(sum_g(nr));
    pq0 = phi1(d[0], sq); pq1 = phi1(d[1], sq);
  }

  float acc[8] = {0.f, 0.f, 0.f, 0.f, 0.f, 0.f, 0.f, 0.f};
  float den = 0.f;
  float* wl = lds + wave * 16 * RING;
  const int wpos = (n & 3) * 4 + (n >> 2);
#pragma unroll
  for (int it = 0; it < NSTEP; ++it) {
    const int r = it % RING;
    const int soff = (it + RING) * SSTRIDE;
    f32x4 d = {0.f, 0.f, 0.f, 0.f}; float nr = 0.f;
#pragma unroll
    for (int kk = 0; kk < 4; ++kk) {
      const u32x4 s = scale8(K[r][kk], SCALE);
      d = mfma16(A[kk], s, d);
#pragma unroll
      for (int j = 0; j < 4; ++j) nr = dot2(s[j], s[j], nr);
    }
    __builtin_amdgcn_sched_barrier(0);
    if (it + RING < NSTEP) {
#pragma unroll
      for (int kk = 0; kk < 4; ++kk) K[r][kk] = __builtin_amdgcn_raw_buffer_load_b128(krs, koff + kk * 64, soff, AUX_NT);
    }
    __builtin_amdgcn_sched_barrier(0);
    const float sq = half_sq(sum_g(nr));
    float w = phi1(d[0], sq) * pq0 + phi1(d[1], sq) * pq1;
    w = sum_g(w);
    den += w;
    wl[r * 16 + wpos] = w;
    asm volatile("" ::: "memory");
    const f32x4 w4 = *reinterpret_cast<const f32x4*>(wl + r * 16 + g * 4);
#pragma unroll
    for (int i = 0; i < 4; ++i) {
#pragma unroll
      for (int j = 0; j < 4; ++j) {
        acc[2 * j] = fmaf(w4[i], bflo(V[r][i][j]), acc[2 * j]);
        acc[2 * j + 1] = fmaf(w4[i], bfhi(V[r][i][j]), acc[2 * j + 1]);
      }
    }
    __builtin_amdgcn_sched_barrier(0);
    if (it + RING < NSTEP) {
#pragma unroll
      for (int i = 0; i < 4; ++i) V[r][i] = __builtin_amdgcn_raw_buffer_load_b128(vrs, voff + i * 1024, soff, AUX_NT);
    }
    __builtin_amdgcn_sched_barrier(0);
  }
#pragma unroll
  for (int j = 0; j < 8; ++j) acc[j] = sum_g(acc[j]);
#pragma unroll
  for (int o = 1; o < 16; o <<= 1) den += __shfl_xor(den, o, 64);
  float* red = lds + NWAVES * 16 * RING + wave * 132;
  if (g == 0) {
    *reinterpret_cast<f32x4*>(red + n * 8) = f32x4{acc[0], acc[1], acc[2], acc[3]};
    *reinterpret_cast<f32x4*>(red + n * 8 + 4) = f32x4{acc[4], acc[5], acc[6], acc[7]};
    if (n == 0) red[128] = den;
  }
  __syncthreads();
  if (wave != 0) return;
  float s0 = 0.f, s1 = 0.f, sd = 0.f;
#pragma unroll
  for (int w = 0; w < NWAVES; ++w) {
    const float* r = lds + NWAVES * 16 * RING + w * 132;
    s0 += r[lane]; s1 += r[lane + 64]; sd += r[128];
  }
  gu64* slots = (gu64*)(ws + WS_SLOTS) + (size_t)bh * NSPLIT * SLOT_G;
  gu64* mine = slots + sp * SLOT_G;
  const unsigned long long th = (unsigned long long)tag << 32;
  __hip_atomic_store(mine + lane, th | __float_as_uint(s0), RLX_AGENT);
  __hip_atomic_store(mine + lane + 64, th | __float_as_uint(s1), RLX_AGENT);
  if (lane == 0) __hip_atomic_store(mine + 128, th | __float_as_uint(sd), RLX_AGENT);
#pragma unroll 1
  for (int pass = 0; pass < 2; ++pass) {
    unsigned long long x0[NSPLIT], x1[NSPLIT], xd[NSPLIT];
#pragma unroll
    for (int j = 0; j < NSPLIT; ++j) {
      x0[j] = __hip_atomic_load(slots + j * SLOT_G + lane, RLX_AGENT);
      x1[j] = __hip_atomic_load(slots + j * SLOT_G + lane + 64, RLX_AGENT);
      xd[j] = __hip_atomic_load(slots + j * SLOT_G + 128, RLX_AGENT);
    }
    unsigned bad = 0; float a0 = 0.f, a1 = 0.f, ad = 0.f;
#pragma unroll
    for (int j = 0; j < NSPLIT; ++j) {
      const bool me = (j == sp);
      const unsigned m = (((unsigned)(x0[j] >> 32)) ^ tag) | (((unsigned)(x1[j] >> 32)) ^ tag) | (((unsigned)(xd[j] >> 32)) ^ tag);
      bad |= me ? 0u : m;
      a0 += me ? s0 : __uint_as_float((unsigned)x0[j]);
      a1 += me ? s1 : __uint_as_float((unsigned)x1[j]);
      ad += me ? sd : __uint_as_float((unsigned)xd[j]);
    }
    if (__all(bad == 0)) {
      const float dn = ad + 1e-6f;
      __bf16 o0 = (__bf16)(a0 / dn), o1 = (__bf16)(a1 / dn);
      out[bh * D_ + lane] = __builtin_bit_cast(unsigned short, o0);
      out[bh * D_ + lane + 64] = __builtin_bit_cast(unsigned short, o1);
      if (lane == 0) __hip_atomic_store(ep, tag, RLX_AGENT);
      return;
    }
    asm volatile("s_waitcnt vmcnt(0)" ::: "memory");
  }
}

extern "C" void kernel_launch(void* const* d_in, const int* in_sizes, int n_in,
                              void* d_out, int out_size, void* d_ws, size_t ws_size,
                              hipStream_t stream) {
  const unsigned short* q = (const unsigned short*)d_in[0];
  const unsigned short* k = (const unsigned short*)d_in[1];
  const unsigned short* v = (const unsigned short*)d_in[2];
  const unsigned short* om = (const unsigned short*)d_in[3];
  rfa_fused<<<BH * NSPLIT, NWAVES * 64, 0, stream>>>(q, k, v, om, (unsigned char*)d_ws, (unsigned short*)d_out);
}
```

```cpp
#include <hip/hip_runtime.h>
#include <hip/hip_bf16.h>
#include <cstdint>

constexpr int B_ = 2, H_ = 32, SK = 4096, D_ = 128, BH = B_ * H_;
constexpr int NSPLIT = 4;
constexpr int NWAVES = 4;
constexpr int RING = 2;
constexpr int KPS = SK / NSPLIT;
constexpr int NSTEP = KPS / NWAVES / 16;
constexpr int SSTRIDE = 4096 * NWAVES;
constexpr int HB = NSPLIT * 1024 + 128;
constexpr size_t WS_SLOTS = 8192;
constexpr int AUX_SC1 = 16;
constexpr int AUX_NT = 2;

using u32x4 = __attribute__((ext_vector_type(4))) unsigned;
using u32x2 = __attribute__((ext_vector_type(2))) unsigned;
using f32x2 = __attribute__((ext_vector_type(2))) float;
using f32x4 = __attribute__((ext_vector_type(4))) float;
using bf16x8 = __attribute__((ext_vector_type(8))) __bf16;
using bf16x2 = __attribute__((ext_vector_type(2))) __bf16;
typedef __attribute__((address_space(1))) unsigned gu32;
typedef __attribute__((address_space(1))) unsigned long long gu64;
#define RLX_AGENT __ATOMIC_RELAXED, __HIP_MEMORY_SCOPE_AGENT

__device__ __forceinline__ float dot2(unsigned a, unsigned b, float acc) {
  return __builtin_amdgcn_fdot2_f32_bf16(__builtin_bit_cast(bf16x2, a), __builtin_bit_cast(bf16x2, b), acc, false);
}
__device__ __forceinline__ f32x4 mfma16(u32x4 a, u32x4 b, f32x4 c) {
  return __builtin_amdgcn_mfma_f32_16x16x32_bf16(__builtin_bit_cast(bf16x8, a), __builtin_bit_cast(bf16x8, b), c, 0, 0, 0);
}
__device__ __forceinline__ float bflo(unsigned u) { return __builtin_bit_cast(float, u << 16); }
__device__ __forceinline__ float bfhi(unsigned u) { return __builtin_bit_cast(float, u & 0xffff0000u); }
__device__ __forceinline__ float rbf(float x) { __bf16 h = (__bf16)x; return (float)h; }
__device__ __forceinline__ unsigned scale_pair(unsigned u, float s) {
  bf16x2 r; r[0] = (__bf16)(bflo(u) * s); r[1] = (__bf16)(bfhi(u) * s);
  return __builtin_bit_cast(unsigned, r);
}
__device__ __forceinline__ u32x4 scale8(u32x4 v, float s) {
  u32x4 r; r[0] = scale_pair(v[0], s); r[1] = scale_pair(v[1], s); r[2] = scale_pair(v[2], s); r[3] = scale_pair(v[3], s);
  return r;
}

constexpr float SCALE = 0.29730177875068026f;
constexpr float INV_SQRT_M = 0.35355339059327373f;
constexpr float LOG2E = 1.4426950408889634f;

__device__ __forceinline__ float phi1(float proj, float sq) {
  float a = rbf(rbf(proj) - sq);
  float e = rbf(__builtin_amdgcn_exp2f(a * LOG2E));
  e = rbf(e * INV_SQRT_M);
  return rbf(e + 1e-6f);
}
__device__ __forceinline__ float half_sq(float nrm) { return 0.5f * rbf(nrm); }
__device__ __forceinline__ float sum_x16(float x) {
  auto s = __builtin_amdgcn_permlane16_swap(__float_as_uint(x), __float_as_uint(x), false, false);
  return __uint_as_float(s[0]) + __uint_as_float(s[1]);
}
__device__ __forceinline__ float sum_x32(float x) {
  auto s = __builtin_amdgcn_permlane32_swap(__float_as_uint(x), __float_as_uint(x), false, false);
  return __uint_as_float(s[0]) + __uint_as_float(s[1]);
}
__device__ __forceinline__ float sum_g(float x) { return sum_x32(sum_x16(x)); }

template <int CTRL> __device__ __forceinline__ float dpp_add(float x) {
  return x + __builtin_bit_cast(float, __builtin_amdgcn_update_dpp(0, __builtin_bit_cast(int, x), CTRL, 0xf, 0xf, false));
}
__device__ __forceinline__ float fold32(float a, float b) {
  auto s = __builtin_amdgcn_permlane32_swap(__float_as_uint(a), __float_as_uint(b), false, false);
  return __uint_as_float(s[0]) + __uint_as_float(s[1]);
}
__device__ __forceinline__ float fold16(float a, float b) {
  auto s = __builtin_amdgcn_permlane16_swap(__float_as_uint(a), __float_as_uint(b), false, false);
  return __uint_as_float(s[0]) + __uint_as_float(s[1]);
}

__global__ void __launch_bounds__(NWAVES * 64, 1)
rfa_fused(const unsigned short* __restrict__ q, const unsigned short* __restrict__ k,
          const unsigned short* __restrict__ v, const unsigned short* __restrict__ om,
          unsigned char* ws, unsigned short* out) {
  __shared__ __attribute__((aligned(16))) float lds[NWAVES * 16 * RING + NWAVES * 132];
  const int tid = threadIdx.x, lane = tid & 63, wave = __builtin_amdgcn_readfirstlane(tid >> 6);
  const int bh = blockIdx.x / NSPLIT, sp = blockIdx.x % NSPLIT;
  const int n = lane & 15, g = lane >> 4;

  const size_t base = ((size_t)bh * SK + (size_t)sp * KPS + (size_t)wave * 16) * D_;
  const auto krs = __builtin_amdgcn_make_buffer_rsrc((void*)(k + base), 0, (NSTEP - 1) * SSTRIDE + 4096, 0x00020000);
  const auto vrs = __builtin_amdgcn_make_buffer_rsrc((void*)(v + base), 0, (NSTEP - 1) * SSTRIDE + 4096, 0x00020000);
  const int koff = n * 256 + g * 16;
  const int voff = lane * 16;

  u32x4 K[RING][4], V[RING][4], A[4], Q[4];
#pragma unroll
  for (int kk = 0; kk < 4; ++kk) K[0][kk] = __builtin_amdgcn_raw_buffer_load_b128(krs, koff + kk * 64, 0, AUX_NT);
  __builtin_amdgcn_sched_barrier(0);
  const int feat = 2 * (n >> 2) + (n & 1);
#pragma unroll
  for (int kk = 0; kk < 4; ++kk) A[kk] = *reinterpret_cast<const u32x4*>(om + feat * D_ + (4 * kk + g) * 8);
#pragma unroll
  for (int kk = 0; kk < 4; ++kk) Q[kk] = *reinterpret_cast<const u32x4*>(q + bh * D_ + (4 * kk + g) * 8);
  gu32* ep = (gu32*)(ws + (size_t)bh * 128);
  const unsigned tag = __hip_atomic_load(ep, RLX_AGENT) + 1u;
  __builtin_amdgcn_sched_barrier(0);
#pragma unroll
  for (int r = 0; r < RING; ++r) {
    if (r > 0) {
#pragma unroll
      for (int kk = 0; kk < 4; ++kk) K[r][kk] = __builtin_amdgcn_raw_buffer_load_b128(krs, koff + kk * 64, r * SSTRIDE, AUX_NT);
    }
#pragma unroll
    for (int i = 0; i < 4; ++i) V[r][i] = __builtin_amdgcn_raw_buffer_load_b128(vrs, voff + i * 1024, r * SSTRIDE, AUX_NT);
  }
  __builtin_amdgcn_sched_barrier(0);

  float pq0, pq1;
  {
    f32x4 d = {0.f, 0.f, 0.f, 0.f}; float nr = 0.f;
#pragma unroll
    for (int kk = 0; kk < 4; ++kk) {
      const u32x4 s = scale8(Q[kk], SCALE);
      d = mfma16(A[kk], s, d);
#pragma unroll
      for (int j = 0; j < 4; ++j) nr = dot2(s[j], s[j], nr);
    }
    const float sq = half_sq(sum_g(nr));
    pq0 = phi1(d[0], sq); pq1 = phi1(d[1], sq);
  }

  unsigned char* hp = ws + WS_SLOTS + (size_t)bh * HB;
  int so[NSPLIT];
#pragma unroll
  for (int j = 0; j < NSPLIT; ++j) so[j] = j == sp ? HB : j * 1024;
  asm volatile("" : "+s"(hp), "+s"(so[0]), "+s"(so[1]), "+s"(so[2]), "+s"(so[3]));
  const auto hrs = __builtin_amdgcn_make_buffer_rsrc((void*)hp, 0, HB, 0x00020000);
  float acc[8] = {0.f, 0.f, 0.f, 0.f, 0.f, 0.f, 0.f, 0.f};
  float den = 0.f;
  float* wl = lds + wave * 16 * RING;
  const int wpos = (n & 3) * 4 + (n >> 2);
#pragma unroll
  for (int it = 0; it < NSTEP; ++it) {
    const int r = it % RING;
    const int soff = (it + RING) * SSTRIDE;
    f32x4 d = {0.f, 0.f, 0.f, 0.f}; float nr = 0.f;
#pragma unroll
    for (int kk = 0; kk < 4; ++kk) {
      const u32x4 s = scale8(K[r][kk], SCALE);
      d = mfma16(A[kk], s, d);
#pragma unroll
      for (int j = 0; j < 4; ++j) nr = dot2(s[j], s[j], nr);
    }
    __builtin_amdgcn_sched_barrier(0);
    if (it + RING < NSTEP) {
#pragma unroll
      for (int kk = 0; kk < 4; ++kk) K[r][kk] = __builtin_amdgcn_raw_buffer_load_b128(krs, koff + kk * 64, soff, AUX_NT);
    }
    __builtin_amdgcn_sched_barrier(0);
    const float sq = half_sq(sum_g(nr));
    float w = phi1(d[0], sq) * pq0 + phi1(d[1], sq) * pq1;
    w = sum_g(w);
    den += w;
    if (it == NSTEP - 1) den = dpp_add<0x121>(dpp_add<0x122>(dpp_add<0x124>(dpp_add<0x128>(den))));
    wl[r * 16 + wpos] = w;
    asm volatile("" ::: "memory");
    const f32x4 w4 = *reinterpret_cast<const f32x4*>(wl + r * 16 + g * 4);
#pragma unroll
    for (int i = 0; i < 4; ++i) {
#pragma unroll
      for (int j = 0; j < 4; ++j) {
        acc[2 * j] = fmaf(w4[i], bflo(V[r][i][j]), acc[2 * j]);
        acc[2 * j + 1] = fmaf(w4[i], bfhi(V[r][i][j]), acc[2 * j + 1]);
      }
    }
    asm volatile("" : "+v"(acc[0]), "+v"(acc[1]), "+v"(acc[2]), "+v"(acc[3]), "+v"(acc[4]), "+v"(acc[5]), "+v"(acc[6]), "+v"(acc[7]), "+v"(den));
    __builtin_amdgcn_sched_barrier(0);
    if (it + RING < NSTEP) {
#pragma unroll
      for (int i = 0; i < 4; ++i) V[r][i] = __builtin_amdgcn_raw_buffer_load_b128(vrs, voff + i * 1024, soff, AUX_NT);
    }
    __builtin_amdgcn_sched_barrier(0);
  }
  const f32x2 part = {fold16(fold32(acc[0], acc[4]), fold32(acc[2], acc[6])), fold16(fold32(acc[1], acc[5]), fold32(acc[3], acc[7]))};
  float* red = lds + NWAVES * 16 * RING + wave * 132;
  *reinterpret_cast<f32x2*>(red + n * 8 + g * 2) = part;
  if (lane == 0) red[128] = den;
  __syncthreads();
  if (wave != 0) return;
  u32x4 x[NSPLIT]; u32x2 xd;
  auto look = [&]() {
    asm volatile("" ::: "memory");
#pragma unroll
    for (int j = 0; j < NSPLIT; ++j)
      x[j] = __builtin_amdgcn_raw_buffer_load_b128(hrs, lane * 16, so[j], AUX_SC1);
    xd = __builtin_amdgcn_raw_buffer_load_b64(hrs, (lane % NSPLIT) * 8, NSPLIT * 1024, AUX_SC1);
    asm volatile("" ::: "memory");
    __builtin_amdgcn_sched_barrier(0);
  };
  auto complete = [&]() {
    unsigned bad = (lane % NSPLIT == sp) ? 0u : xd[1] ^ tag;
#pragma unroll
    for (int j = 0; j < NSPLIT; ++j) bad |= ((x[j][1] ^ tag) | (x[j][3] ^ tag)) & (j == sp ? 0u : ~0u);
    return __all(bad == 0);
  };
  look();
  f32x2 s = {0.f, 0.f}; float sd = 0.f;
#pragma unroll
  for (int w = 0; w < NWAVES; ++w) {
    const float* r = lds + NWAVES * 16 * RING + w * 132;
    s += *reinterpret_cast<const f32x2*>(r + 2 * lane); sd += r[128];
  }
  __builtin_amdgcn_raw_buffer_store_b128(u32x4{__float_as_uint(s[0]), tag, __float_as_uint(s[1]), tag}, hrs, lane * 16, sp * 1024, AUX_SC1);
  __builtin_amdgcn_raw_buffer_store_b64(u32x2{__float_as_uint(sd), tag}, hrs, lane == 0 ? sp * 8 : HB, NSPLIT * 1024, AUX_SC1);
  __builtin_amdgcn_sched_barrier(0);
  if (!complete()) {
    asm volatile("s_waitcnt vmcnt(0)" ::: "memory");
    look();
    if (!complete()) return;
  }
  float a0 = 0.f, a1 = 0.f, ad = 0.f;
  const unsigned dv = (lane % NSPLIT == sp) ? __float_as_uint(sd) : xd[0];
#pragma unroll
  for (int j = 0; j < NSPLIT; ++j) {
    const unsigned m = j == sp ? ~0u : 0u;
    a0 += __uint_as_float((x[j][0] & ~m) | (__float_as_uint(s[0]) & m));
    a1 += __uint_as_float((x[j][2] & ~m) | (__float_as_uint(s[1]) & m));
    ad += __uint_as_float(__builtin_amdgcn_readlane(dv, j));
  }
  const float dn = ad + 1e-6f;
  const float sc = dn > 0x1p100f ? 0x1p-32f : 1.f;
  const float rn = __builtin_amdgcn_rcpf(dn * sc);
  bf16x2 o2; o2[0] = (__bf16)(a0 * sc * rn); o2[1] = (__bf16)(a1 * sc * rn);
  const auto ors = __builtin_amdgcn_make_buffer_rsrc((void*)(out + bh * D_), 0, D_ * 2, 0x00020000);
  __builtin_amdgcn_raw_buffer_store_b32(__builtin_bit_cast(unsigned, o2), ors, lane * 4, 0, AUX_SC1);
  if (lane == 0) __hip_atomic_store(ep, tag, RLX_AGENT);
}

extern "C" void kernel_launch(void* const* d_in, const int* in_sizes, int n_in,
                              void* d_out, int out_size, void* d_ws, size_t ws_size,
                              hipStream_t stream) {
  const unsigned short* q = (const unsigned short*)d_in[0];
  const unsigned short* k = (const unsigned short*)d_in[1];
  const unsigned short* v = (const unsigned short*)d_in[2];
  const unsigned short* om = (const unsigned short*)d_in[3];
  rfa_fused<<<BH * NSPLIT, NWAVES * 64, 0, stream>>>(q, k, v, om, (unsigned char*)d_ws, (unsigned short*)d_out);
}
```

```cpp
#include <hip/hip_runtime.h>
#include <hip/hip_bf16.h>
#include <cstdint>

constexpr int B_ = 2, H_ = 32, SK = 4096, D_ = 128, BH = B_ * H_;
constexpr int NSPLIT = 4;
constexpr int NWAVES = 4;
constexpr int RING = 2;
constexpr int KPS = SK / NSPLIT;
constexpr int NSTEP = KPS / NWAVES / 16;
static_assert(NSTEP % RING == 0 && NSPLIT == 4, "tile ring / hand-off layout");
constexpr int SSTRIDE = 4096 * NWAVES;
constexpr int HB = NSPLIT * 1024 + 128;
constexpr size_t WS_SLOTS = 8192;
constexpr int AUX_SC1 = 16;
constexpr int AUX_NT = 2;

using u32x4 = __attribute__((ext_vector_type(4))) unsigned;
using u32x2 = __attribute__((ext_vector_type(2))) unsigned;
using f32x2 = __attribute__((ext_vector_type(2))) float;
using f32x4 = __attribute__((ext_vector_type(4))) float;
using bf16x8 = __attribute__((ext_vector_type(8))) __bf16;
using bf16x2 = __attribute__((ext_vector_type(2))) __bf16;
typedef __attribute__((address_space(1))) unsigned gu32;
#define RLX_AGENT __ATOMIC_RELAXED, __HIP_MEMORY_SCOPE_AGENT

__device__ __forceinline__ float dot2(unsigned a, unsigned b, float acc) {
  return __builtin_amdgcn_fdot2_f32_bf16(__builtin_bit_cast(bf16x2, a), __builtin_bit_cast(bf16x2, b), acc, false);
}
__device__ __forceinline__ f32x4 mfma16(u32x4 a, u32x4 b, f32x4 c) {
  return __builtin_amdgcn_mfma_f32_16x16x32_bf16(__builtin_bit_cast(bf16x8, a), __builtin_bit_cast(bf16x8, b), c, 0, 0, 0);
}
__device__ __forceinline__ float bflo(unsigned u) { return __builtin_bit_cast(float, u << 16); }
__device__ __forceinline__ float bfhi(unsigned u) { return __builtin_bit_cast(float, u & 0xffff0000u); }
__device__ __forceinline__ float rbf(float x) { __bf16 h = (__bf16)x; return (float)h; }
__device__ __forceinline__ f32x2 unpack2(unsigned u) { return f32x2{bflo(u), bfhi(u)}; }
__device__ __forceinline__ unsigned pack2(f32x2 x) {
  bf16x2 r; r[0] = (__bf16)x[0]; r[1] = (__bf16)x[1];
  return __builtin_bit_cast(unsigned, r);
}
__device__ __forceinline__ f32x2 rbf2(f32x2 x) { unsigned u = pack2(x); asm("" : "+v"(u)); return unpack2(u); }
__device__ __forceinline__ unsigned scale_pair(unsigned u, float s) { return pack2(unpack2(u) * s); }
__device__ __forceinline__ u32x4 scale8(u32x4 v, float s) {
  u32x4 r; r[0] = scale_pair(v[0], s); r[1] = scale_pair(v[1], s); r[2] = scale_pair(v[2], s); r[3] = scale_pair(v[3], s);
  return r;
}

constexpr float SCALE = 0.29730177875068026f;
constexpr float INV_SQRT_M = 0.35355339059327373f;
constexpr float LOG2E = 1.4426950408889634f;

__device__ __forceinline__ unsigned phi2(f32x2 proj, f32x2 sq) {
  const f32x2 a = rbf2(rbf2(proj) - sq) * LOG2E;
  f32x2 e = rbf2(f32x2{__builtin_amdgcn_exp2f(a[0]), __builtin_amdgcn_exp2f(a[1])});
  e = rbf2(e * INV_SQRT_M);
  return pack2(e + 1e-6f);
}
__device__ __forceinline__ float half_sq(float nrm) { return 0.5f * rbf(nrm); }
__device__ __forceinline__ float sum_x16(float x) {
  auto s = __builtin_amdgcn_permlane16_swap(__float_as_uint(x), __float_as_uint(x), false, false);
  return __uint_as_float(s[0]) + __uint_as_float(s[1]);
}
__device__ __forceinline__ float sum_x32(float x) {
  auto s = __builtin_amdgcn_permlane32_swap(__float_as_uint(x), __float_as_uint(x), false, false);
  return __uint_as_float(s[0]) + __uint_as_float(s[1]);
}
__device__ __forceinline__ float sum_g(float x) { return sum_x32(sum_x16(x)); }

template <int CTRL> __device__ __forceinline__ float dpp_add(float x) {
  return x + __builtin_bit_cast(float, __builtin_amdgcn_update_dpp(0, __builtin_bit_cast(int, x), CTRL, 0xf, 0xf, false));
}
__device__ __forceinline__ float fold32(float a, float b) {
  auto s = __builtin_amdgcn_permlane32_swap(__float_as_uint(a), __float_as_uint(b), false, false);
  return __uint_as_float(s[0]) + __uint_as_float(s[1]);
}
__device__ __forceinline__ float fold16(float a, float b) {
  auto s = __builtin_amdgcn_permlane16_swap(__float_as_uint(a), __float_as_uint(b), false, false);
  return __uint_as_float(s[0]) + __uint_as_float(s[1]);
}

__global__ void __launch_bounds__(NWAVES * 64, 1)
rfa_fused(const unsigned short* __restrict__ q, const unsigned short* __restrict__ k,
          const unsigned short* __restrict__ v, const unsigned short* __restrict__ om,
          unsigned char* ws, unsigned short* out) {
  __shared__ __attribute__((aligned(16))) float lds[NWAVES * 16 * RING + NWAVES * 132];
  const int tid = threadIdx.x, lane = tid & 63, wave = __builtin_amdgcn_readfirstlane(tid >> 6);
  const int bh = blockIdx.x / NSPLIT, sp = blockIdx.x % NSPLIT;
  const int n = lane & 15, g = lane >> 4;

  const size_t base = ((size_t)bh * SK + (size_t)sp * KPS + (size_t)wave * 16) * D_;
  const auto krs = __builtin_amdgcn_make_buffer_rsrc((void*)(k + base), 0, (NSTEP - 1) * SSTRIDE + 4096, 0x00020000);
  const auto vrs = __builtin_amdgcn_make_buffer_rsrc((void*)(v + base), 0, (NSTEP - 1) * SSTRIDE + 4096, 0x00020000);
  const int koff = ((n & 3) * 4 + (n >> 2)) * 256 + g * 16;
  const int voff = lane * 16;

  u32x4 K[RING][4], V[RING][4], A[4], Q[4];
#pragma unroll
  for (int kk = 0; kk < 4; ++kk) K[0][kk] = __builtin_amdgcn_raw_buffer_load_b128(krs, koff + kk * 64, 0, AUX_NT);
  __builtin_amdgcn_sched_barrier(0);
  const int feat = n & 7;
#pragma unroll
  for (int kk = 0; kk < 4; ++kk) A[kk] = *reinterpret_cast<const u32x4*>(om + feat * D_ + (4 * kk + g) * 8);
#pragma unroll
  for (int kk = 0; kk < 4; ++kk) Q[kk] = *reinterpret_cast<const u32x4*>(q + bh * D_ + (4 * kk + g) * 8);
  gu32* ep = (gu32*)(ws + (size_t)bh * 128);
  const unsigned tag = __hip_atomic_load(ep, RLX_AGENT) + 1u;
  __builtin_amdgcn_sched_barrier(0);
#pragma unroll
  for (int r = 0; r < RING; ++r) {
    if (r > 0) {
#pragma unroll
      for (int kk = 0; kk < 4; ++kk) {
        K[r][kk] = __builtin_amdgcn_raw_buffer_load_b128(krs, koff + kk * 64, r * SSTRIDE, AUX_NT);
        __builtin_amdgcn_sched_barrier(0);
      }
    }
#pragma unroll
    for (int i = 0; i < 4; ++i) V[r][i] = __builtin_amdgcn_raw_buffer_load_b128(vrs, voff + i * 1024, r * SSTRIDE, AUX_NT);
  }
  __builtin_amdgcn_sched_barrier(0);

  unsigned char* hp = ws + WS_SLOTS + (size_t)bh * HB;
  int so[NSPLIT];
#pragma unroll
  for (int j = 0; j < NSPLIT; ++j) so[j] = j == sp ? HB : j * 1024;
  asm volatile("" : "+s"(hp), "+s"(so[0]), "+s"(so[1]), "+s"(so[2]), "+s"(so[3]));
  const auto hrs = __builtin_amdgcn_make_buffer_rsrc((void*)hp, 0, HB, 0x00020000);
  f32x4 kv[8];
#pragma unroll
  for (int j = 0; j < 8; ++j) kv[j] = f32x4{0.f, 0.f, 0.f, 0.f};
  float z = 0.f;
  float* wl = lds + wave * 16 * RING;
  const bool hi = n >= 8;
  const unsigned m0 = hi ? 0u : ~0u;
  float den = 0.f;
  float pqn;
  f32x2 pq01, pq23;
  auto step = [&](const int r, const int soff, const bool more, const bool last) __attribute__((always_inline)) {
    f32x4 d = {0.f, 0.f, 0.f, 0.f}; float nr = 0.f;
#pragma unroll
    for (int kk = 0; kk < 4; ++kk) {
      const u32x4 s = scale8(K[r][kk], SCALE);
      d = mfma16(s, A[kk], d);
#pragma unroll
      for (int j = 0; j < 4; ++j) nr = dot2(s[j], s[j], nr);
    }
    __builtin_amdgcn_sched_barrier(0);
    if (more) {
#pragma unroll
      for (int kk = 0; kk < 4; ++kk) K[r][kk] = __builtin_amdgcn_raw_buffer_load_b128(krs, koff + kk * 64, soff, AUX_NT);
    }
    __builtin_amdgcn_sched_barrier(0);
    wl[r * 16 + n] = half_sq(sum_g(nr));
    asm volatile("" ::: "memory");
    const f32x2 sq = *reinterpret_cast<const f32x2*>(wl + r * 16 + g * 4 + (hi ? 2 : 0));
    const unsigned p = phi2(f32x2{hi ? d[2] : d[0], hi ? d[3] : d[1]}, sq);
    z = dot2(p, 0x3f803f80u, z);
    if (last) {
      den = dpp_add<0x121>(dpp_add<0x122>(dpp_add<0x124>(dpp_add<0x128>(sum_g(z * pqn)))));
      asm volatile("" : "+v"(den));
    }
    const u32x2 pa = {p & m0, p & ~m0};
    asm volatile(
        "v_perm_b32 v140, %[b0], %[a0], %[sl]\n\t"
        "v_perm_b32 v141, %[d0], %[c0], %[sl]\n\t"
        "v_perm_b32 v142, %[b0], %[a0], %[sh]\n\t"
        "v_perm_b32 v143, %[d0], %[c0], %[sh]\n\t"
        "v_mfma_f32_16x16x16_bf16 %[k0], %[pa], v[140:141], %[k0]\n\t"
        "v_perm_b32 v144, %[b1], %[a1], %[sl]\n\t"
        "v_perm_b32 v145, %[d1], %[c1], %[sl]\n\t"
        "v_mfma_f32_16x16x16_bf16 %[k1], %[pa], v[142:143], %[k1]\n\t"
        "v_perm_b32 v140, %[b1], %[a1], %[sh]\n\t"
        "v_perm_b32 v141, %[d1], %[c1], %[sh]\n\t"
        "v_mfma_f32_16x16x16_bf16 %[k2], %[pa], v[144:145], %[k2]\n\t"
        "v_perm_b32 v142, %[b2], %[a2], %[sl]\n\t"
        "v_perm_b32 v143, %[d2], %[c2], %[sl]\n\t"
        "v_mfma_f32_16x16x16_bf16 %[k3], %[pa], v[140:141], %[k3]\n\t"
        "v_perm_b32 v144, %[b2], %[a2], %[sh]\n\t"
        "v_perm_b32 v145, %[d2], %[c2], %[sh]\n\t"
        "v_mfma_f32_16x16x16_bf16 %[k4], %[pa], v[142:143], %[k4]\n\t"
        "v_perm_b32 v140, %[b3], %[a3], %[sl]\n\t"
        "v_perm_b32 v141, %[d3], %[c3], %[sl]\n\t"
        "v_mfma_f32_16x16x16_bf16 %[k5], %[pa], v[144:145], %[k5]\n\t"
        "v_perm_b32 v142, %[b3], %[a3], %[sh]\n\t"
        "v_perm_b32 v143, %[d3], %[c3], %[sh]\n\t"
        "v_mfma_f32_16x16x16_bf16 %[k6], %[pa], v[140:141], %[k6]\n\t"
        "s_nop 0\n\t"
        "v_mfma_f32_16x16x16_bf16 %[k7], %[pa], v[142:143], %[k7]\n\t"
        "s_nop 7\n\t"
        "s_nop 7\n\t"
        : [k0] "+v"(kv[0]), [k1] "+v"(kv[1]), [k2] "+v"(kv[2]), [k3] "+v"(kv[3]), [k4] "+v"(kv[4]), [k5] "+v"(kv[5]), [k6] "+v"(kv[6]), [k7] "+v"(kv[7])
        : [pa] "v"(pa), [sl] "s"(0x05040100u), [sh] "s"(0x07060302u),
          [a0] "v"(V[r][0][0]), [a1] "v"(V[r][0][1]), [a2] "v"(V[r][0][2]), [a3] "v"(V[r][0][3]), [b0] "v"(V[r][1][0]), [b1] "v"(V[r][1][1]), [b2] "v"(V[r][1][2]), [b3] "v"(V[r][1][3]), [c0] "v"(V[r][2][0]), [c1] "v"(V[r][2][1]), [c2] "v"(V[r][2][2]), [c3] "v"(V[r][2][3]), [d0] "v"(V[r][3][0]), [d1] "v"(V[r][3][1]), [d2] "v"(V[r][3][2]), [d3] "v"(V[r][3][3])
        : "v140", "v141", "v142", "v143", "v144", "v145");
    __builtin_amdgcn_sched_barrier(0);
    if (more) {
#pragma unroll
      for (int i = 0; i < 4; ++i) V[r][i] = __builtin_amdgcn_raw_buffer_load_b128(vrs, voff + i * 1024, soff, AUX_NT);
    }
    __builtin_amdgcn_sched_barrier(0);
  };
#pragma unroll 1
  for (int soff = RING * SSTRIDE; soff < NSTEP * SSTRIDE; soff += RING * SSTRIDE) {
#pragma unroll
    for (int r = 0; r < RING; ++r) step(r, soff + r * SSTRIDE, true, false);
  }
  {
    f32x4 d = {0.f, 0.f, 0.f, 0.f}, dt = d; float nr = 0.f;
#pragma unroll
    for (int kk = 0; kk < 4; ++kk) {
      const u32x4 s = scale8(Q[kk], SCALE);
      d = mfma16(A[kk], s, d);
      dt = mfma16(s, A[kk], dt);
#pragma unroll
      for (int j = 0; j < 4; ++j) nr = dot2(s[j], s[j], nr);
    }
    const float sq = half_sq(sum_g(nr));
    pq01 = unpack2(phi2(f32x2{d[0], d[1]}, f32x2{sq, sq})); pq23 = unpack2(phi2(f32x2{d[2], d[3]}, f32x2{sq, sq}));
    pqn = bflo(phi2(f32x2{dt[0], dt[0]}, f32x2{sq, sq}));
  }
  __builtin_amdgcn_sched_barrier(0);
#pragma unroll
  for (int r = 0; r < RING; ++r) step(r, 0, false, r == RING - 1);
  float acc[8];
#pragma unroll
  for (int j = 0; j < 8; ++j) {
    const f32x2 t = f32x2{kv[j][0], kv[j][1]} * pq01 + f32x2{kv[j][2], kv[j][3]} * pq23;
    acc[j] = t[0] + t[1];
  }
  const f32x2 part = {fold16(fold32(acc[0], acc[4]), fold32(acc[2], acc[6])), fold16(fold32(acc[1], acc[5]), fold32(acc[3], acc[7]))};
  float* red = lds + NWAVES * 16 * RING + wave * 132;
  *reinterpret_cast<f32x2*>(red + n * 8 + g * 2) = part;
  if (lane == 0) red[128] = den;
  __syncthreads();
  if (wave != 0) return;
  u32x4 x[NSPLIT]; u32x2 xd;
  auto look = [&]() {
    asm volatile("" ::: "memory");
#pragma unroll
    for (int j = 0; j < NSPLIT; ++j)
      x[j] = __builtin_amdgcn_raw_buffer_load_b128(hrs, lane * 16, so[j], AUX_SC1);
    xd = __builtin_amdgcn_raw_buffer_load_b64(hrs, (lane % NSPLIT) * 8, NSPLIT * 1024, AUX_SC1);
    asm volatile("" ::: "memory");
    __builtin_amdgcn_sched_barrier(0);
  };
  auto complete = [&]() {
    unsigned bad = (lane % NSPLIT == sp) ? 0u : xd[1] ^ tag;
#pragma unroll
    for (int j = 0; j < NSPLIT; ++j) bad |= ((x[j][1] ^ tag) | (x[j][3] ^ tag)) & (j == sp ? 0u : ~0u);
    return __all(bad == 0);
  };
  look();
  f32x2 s = {0.f, 0.f}; float sd = 0.f;
#pragma unroll
  for (int w = 0; w < NWAVES; ++w) {
    const float* r = lds + NWAVES * 16 * RING + w * 132;
    s += *reinterpret_cast<const f32x2*>(r + 2 * lane); sd += r[128];
  }
  __builtin_amdgcn_raw_buffer_store_b128(u32x4{__float_as_uint(s[0]), tag, __float_as_uint(s[1]), tag}, hrs, lane * 16, sp * 1024, AUX_SC1);
  __builtin_amdgcn_raw_buffer_store_b64(u32x2{__float_as_uint(sd), tag}, hrs, lane == 0 ? sp * 8 : HB, NSPLIT * 1024, AUX_SC1);
  __builtin_amdgcn_sched_barrier(0);
  if (!complete()) {
    asm volatile("s_waitcnt vmcnt(0)" ::: "memory");
    look();
    if (!complete()) return;
  }
  float a0 = 0.f, a1 = 0.f, ad = 0.f;
  const unsigned dv = (lane % NSPLIT == sp) ? __float_as_uint(sd) : xd[0];
#pragma unroll
  for (int j = 0; j < NSPLIT; ++j) {
    const unsigned m = j == sp ? ~0u : 0u;
    a0 += __uint_as_float((x[j][0] & ~m) | (__float_as_uint(s[0]) & m));
    a1 += __uint_as_float((x[j][2] & ~m) | (__float_as_uint(s[1]) & m));
    ad += __uint_as_float(__builtin_amdgcn_readlane(dv, j));
  }
  const float dn = ad + 1e-6f;
  const float sc = dn > 0x1p100f ? 0x1p-32f : 1.f;
  const float rn = __builtin_amdgcn_rcpf(dn * sc);
  bf16x2 o2; o2[0] = (__bf16)(a0 * sc * rn); o2[1] = (__bf16)(a1 * sc * rn);
  const auto ors = __builtin_amdgcn_make_buffer_rsrc((void*)(out + bh * D_), 0, D_ * 2, 0x00020000);
  __builtin_amdgcn_raw_buffer_store_b32(__builtin_bit_cast(unsigned, o2), ors, lane * 4, 0, AUX_SC1);
  if (lane == 0) __hip_atomic_store(ep, tag, RLX_AGENT);
}

extern "C" void kernel_launch(void* const* d_in, const int* in_sizes, int n_in,
                              void* d_out, int out_size, void* d_ws, size_t ws_size,
                              hipStream_t stream) {
  const unsigned short* q = (const unsigned short*)d_in[0];
  const unsigned short* k = (const unsigned short*)d_in[1];
  const unsigned short* v = (const unsigned short*)d_in[2];
  const unsigned short* om = (const unsigned short*)d_in[3];
  rfa_fused<<<BH * NSPLIT, NWAVES * 64, 0, stream>>>(q, k, v, om, (unsigned char*)d_ws, (unsigned short*)d_out);
}
```
